```python
import torch

def _softmax(x, axis=-1):
    return torch.softmax(x, dim=axis)
def _gelu(x):
    return torch.nn.functional.gelu(x, approximate="tanh")
def _silu(x):
    return torch.nn.functional.silu(x)
def _sigmoid(x):
    return torch.sigmoid(x)
def _layernorm(x, axis=-1, eps=1e-5):
    m = x.mean(dim=axis, keepdim=True)
    v = x.var(dim=axis, keepdim=True, unbiased=False)
    return (x - m) / torch.sqrt(v + eps)
def _scatter_add(tgt, idx, src, axis=0):
    return tgt.clone().index_add_(axis, idx, src)
def _segment_sum(src, seg, n):
    out = torch.zeros((n,) + src.shape[1:], dtype=src.dtype)
    return out.index_add_(0, seg, src)
def _pad(x, pw):
    flat = tuple(v for pair in reversed(pw) for v in pair)
    return torch.nn.functional.pad(x, flat)

kernel_name = 'gated_mix__mha_block_cached__dense_activation_silu__decode'

def setup_inputs():

    _w = (lambda a: (lambda t: t.to(torch.bfloat16) if t.is_floating_point() else t)(torch.as_tensor(a)))
    def _A_setup():
        B, Sq, D = 2, 1, 512
        H, Sk, Dh = 32, 8192, 128
        return {
            "x": _w((lambda *s: torch.randn(*s, dtype=torch.bfloat16) * 0.05)(B, Sq, D)),
            "k_cache": _w((lambda *s: torch.randn(*s, dtype=torch.bfloat16) * 3.2)(B, H, Sk, Dh)),
            "v_cache": _w((lambda *s: torch.randn(*s, dtype=torch.bfloat16) * 0.1)(B, H, Sk, Dh)),
            "wq": _w((lambda *s: torch.randn(*s, dtype=torch.bfloat16) * 3.2)(D, H * Dh) * (D ** -0.5)),
            "wo": _w((lambda *s: torch.randn(*s, dtype=torch.bfloat16) * 0.1)(H * Dh, D) * ((H * Dh) ** -0.5)),
        }
    _A_in = _A_setup()

    def _B_setup():
        B, S, D, F = 2, 1, 512, 1792
        return {
            "x": _w((lambda *s: torch.randn(*s, dtype=torch.bfloat16) * 0.05)(B, S, D)),
            "w1": _w((lambda *s: torch.randn(*s, dtype=torch.bfloat16) * 0.05)(D, F) * (D ** -0.5)),
            "w2": _w((lambda *s: torch.randn(*s, dtype=torch.bfloat16) * 0.05)(F, D) * (F ** -0.5)),
        }
    _B_in = _B_setup()

    return {
        "A_x": _A_in["x"],
        "A_k_cache": _A_in["k_cache"],
        "A_v_cache": _A_in["v_cache"],
        "A_wq": _A_in["wq"],
        "A_wo": _A_in["wo"],
        "B_x": _B_in["x"],
        "B_w1": _B_in["w1"],
        "B_w2": _B_in["w2"],
        "gate_w": _w((lambda *s: torch.randn(*s, dtype=torch.bfloat16) * 0.05)(512) * 16.0),
    }

def reference(A_x, A_k_cache, A_v_cache, A_wq, A_wo, B_x, B_w1, B_w2, gate_w):
    def _A_ref(x, k_cache, v_cache, wq, wo):
        B, Sq, D = x.shape
        H, Dh = 32, 128
        q = (lambda x, *d: x.permute(*d))((x @ wq).reshape(B, Sq, H, Dh), 0, 2, 1, 3)
        scores = torch.einsum("bhqd,bhkd->bhqk", q, k_cache) * (Dh ** -0.5)
        p = _softmax(scores, axis=-1)
        o = torch.einsum("bhqk,bhkd->bhqd", p, v_cache)
        return (lambda x, *d: x.permute(*d))(o, 0, 2, 1, 3).reshape(B, Sq, H * Dh) @ wo

    def _B_ref(x, w1, w2):
        h = torch.einsum("bsd,df->bsf", x, w1)
        h = _silu(h)
        out = torch.einsum("bsf,fd->bsd", h, w2)
        return out

    x = A_x
    g = _sigmoid((x * gate_w).sum(-1, keepdims=True))
    a = _A_ref(x, A_k_cache, A_v_cache, A_wq, A_wo)
    b = _B_ref(x, B_w1, B_w2)
    return g * a + (1 - g) * b
```

```cpp
#include <hip/hip_runtime.h>
#include <stdint.h>

typedef unsigned short u16;
typedef unsigned long long u64;
typedef unsigned v4u __attribute__((ext_vector_type(4)));
typedef unsigned v2u __attribute__((ext_vector_type(2)));
typedef float v4f __attribute__((ext_vector_type(4)));
typedef float v2f __attribute__((ext_vector_type(2)));
typedef __bf16 bf16x2 __attribute__((ext_vector_type(2)));
#define GAS __attribute__((address_space(1)))
#define RLX_AGENT __ATOMIC_RELAXED, __HIP_MEMORY_SCOPE_AGENT

#define KV_AUX 2

constexpr int DM = 512, NH = 32, SK = 8192, DH = 128, FF = 1792;
constexpr int NWG = 256, NTHR = 512;
constexpr int NGRP = 16, RING = 3;
constexpr int NFIN = 32, NZ = 224;
constexpr int WS_READY = 256, WS_SLOT = 4096;
constexpr int Y_BYTES = 128 * 128, Z_BYTES = NZ * 128, ML_BYTES = 128 * 8;
constexpr int FIN_BYTES = Y_BYTES + Z_BYTES + ML_BYTES;
constexpr int WS_BYTES = WS_SLOT + NFIN * FIN_BYTES;
constexpr unsigned SENT = 0xFFFFFFFFu;
constexpr int AUX_SC1 = 16;
constexpr int RSRC_W3 = 0x00020000;
constexpr unsigned SPIN_CAP = 1u << 15;

__device__ __forceinline__ float bf_lo(unsigned u) { return __builtin_bit_cast(float, u << 16); }
__device__ __forceinline__ float bf_hi(unsigned u) { return __builtin_bit_cast(float, u & 0xffff0000u); }
__device__ __forceinline__ float dot2(unsigned a, unsigned b, float c) {
  return __builtin_amdgcn_fdot2_f32_bf16(__builtin_bit_cast(bf16x2, a), __builtin_bit_cast(bf16x2, b), c, false);
}
__device__ __forceinline__ unsigned pack_bf16(float lo, float hi) {
  bf16x2 v;
  v.x = (__bf16)lo;
  v.y = (__bf16)hi;
  return __builtin_bit_cast(unsigned, v);
}
__device__ __forceinline__ float round_bf16(float f) { return bf_lo(pack_bf16(f, 0.f) & 0xffffu); }
template <int CTRL> __device__ __forceinline__ float dpp(float x) {
  return __builtin_bit_cast(float, __builtin_amdgcn_mov_dpp(__builtin_bit_cast(int, x), CTRL, 0xf, 0xf, true));
}
constexpr int XOR1 = 0xB1, XOR2 = 0x4E, XOR7 = 0x141, XOR8 = 0x128, BCAST = 0x150;
__device__ __forceinline__ float xrow_max(float x) {
  auto s = __builtin_amdgcn_permlane16_swap(__float_as_uint(x), __float_as_uint(x), false, false);
  x = fmaxf(__uint_as_float(s[0]), __uint_as_float(s[1]));
  auto t = __builtin_amdgcn_permlane32_swap(__float_as_uint(x), __float_as_uint(x), false, false);
  return fmaxf(__uint_as_float(t[0]), __uint_as_float(t[1]));
}
__device__ __forceinline__ float xrow_sum(float x) {
  auto s = __builtin_amdgcn_permlane16_swap(__float_as_uint(x), __float_as_uint(x), false, false);
  x = __uint_as_float(s[0]) + __uint_as_float(s[1]);
  auto t = __builtin_amdgcn_permlane32_swap(__float_as_uint(x), __float_as_uint(x), false, false);
  return __uint_as_float(t[0]) + __uint_as_float(t[1]);
}
__device__ __forceinline__ float row_max(float x) {
  x = fmaxf(x, dpp<XOR8>(x));
  x = fmaxf(x, dpp<XOR7>(x));
  x = fmaxf(x, dpp<XOR2>(x));
  x = fmaxf(x, dpp<XOR1>(x));
  return x;
}
__device__ __forceinline__ float row_sum(float x) {
  x += dpp<XOR8>(x);
  x += dpp<XOR7>(x);
  x += dpp<XOR2>(x);
  x += dpp<XOR1>(x);
  return x;
}
__device__ __forceinline__ float transpose_reduce16(float (&x)[16], int c) {
#define TR_STEP(HALF, CTRL, BIT)                                         \
  _Pragma("unroll") for (int h = 0; h < (HALF); ++h) {                   \
    const float lo_ = x[h], hi_ = x[h + (HALF)];                         \
    const float t1_ = lo_ + dpp<CTRL>(lo_), t2_ = hi_ + dpp<CTRL>(hi_);  \
    x[h] = (c & (BIT)) ? t2_ : t1_;                                      \
  }
  TR_STEP(8, XOR8, 8) TR_STEP(4, XOR7, 4) TR_STEP(2, XOR2, 2) TR_STEP(1, XOR1, 1)
#undef TR_STEP
  return x[0];
}
__device__ __forceinline__ void wg_barrier() { asm volatile("s_waitcnt lgkmcnt(0)\n\ts_barrier" ::: "memory"); }
__device__ __forceinline__ unsigned canon(float f) {
  const unsigned u = __float_as_uint(f);
  return u == SENT ? 0x7FC00000u : u;
}
#define SCHED_FENCE() __builtin_amdgcn_sched_barrier(0)

template <int I, int N, int STRIDE> struct BcastFma {
  static __device__ __forceinline__ void run(float p, const v4u (&v)[N], float (&acc)[8]) {
    const float pb = dpp<BCAST + I * STRIDE>(p);
#pragma unroll
    for (int k = 0; k < 4; ++k) {
      acc[2 * k] = fmaf(pb, bf_lo(v[I][k]), acc[2 * k]);
      acc[2 * k + 1] = fmaf(pb, bf_hi(v[I][k]), acc[2 * k + 1]);
    }
    asm volatile("" : "+v"(acc[0]), "+v"(acc[1]), "+v"(acc[2]), "+v"(acc[3]), "+v"(acc[4]), "+v"(acc[5]), "+v"(acc[6]), "+v"(acc[7]));
    BcastFma<I + 1, N, STRIDE>::run(p, v, acc);
  }
};
template <int N, int STRIDE> struct BcastFma<N, N, STRIDE> {
  static __device__ __forceinline__ void run(float, const v4u (&)[N], float (&)[8]) {}
};
__device__ __forceinline__ float transpose_reduce8(float (&x)[8], int c) {
#define TR_STEP(HALF, CTRL, BIT)                                         \
  _Pragma("unroll") for (int h = 0; h < (HALF); ++h) {                   \
    const float lo_ = x[h], hi_ = x[h + (HALF)];                         \
    const float t1_ = lo_ + dpp<CTRL>(lo_), t2_ = hi_ + dpp<CTRL>(hi_);  \
    x[h] = (c & (BIT)) ? t2_ : t1_;                                      \
  }
  TR_STEP(4, XOR8, 8) TR_STEP(2, XOR7, 4) TR_STEP(1, XOR2, 2)
#undef TR_STEP
  return x[0] + dpp<XOR1>(x[0]);
}

__device__ __forceinline__ void issue_group(__amdgpu_buffer_rsrc_t rs, int voff, int soff, v4u (&dst)[8]) {
#pragma unroll
  for (int i = 0; i < 8; ++i) {
    dst[i] = __builtin_amdgcn_raw_buffer_load_b128(rs, voff + (i & 3) * 1024, soff + (i >> 2) * 4096, KV_AUX);
    SCHED_FENCE();
  }
}
#define PIN4(v) asm volatile("" : "+v"((v)[0]), "+v"((v)[1]), "+v"((v)[2]), "+v"((v)[3]))
#define PIN8(v) asm volatile("" : "+v"((v)[0]), "+v"((v)[1]), "+v"((v)[2]), "+v"((v)[3]), "+v"((v)[4]), "+v"((v)[5]), "+v"((v)[6]), "+v"((v)[7]))
constexpr int OOB = 0x7FFF0000;

constexpr int L_QP = 0;
constexpr int L_HP = L_QP + 8 * 128;
constexpr int L_Q16 = L_HP + 8 * 16;
constexpr int L_HS = L_Q16 + 64;
constexpr int L_O = L_HS + 16;
constexpr int L_M = L_O + 8 * 128;
constexpr int L_L = L_M + 8;
constexpr int L_Y = L_L + 8;
constexpr int L_FIN = L_Y + 8 * 512;
constexpr int L_TOTAL = L_FIN + 8 * 32;

__global__ void __launch_bounds__(NTHR, 2)
fused_decode_kernel(const u16* __restrict__ x, const u16* __restrict__ kc, const u16* __restrict__ vc,
                    const u16* __restrict__ wq, const u16* __restrict__ wo, const u16* __restrict__ w1,
                    const u16* __restrict__ w2, const u16* __restrict__ gw, u16* __restrict__ out,
                    unsigned char* ws) {
  __shared__ __attribute__((aligned(16))) float lds[L_TOTAL];
  const int tid = threadIdx.x, lane = tid & 63, w = __builtin_amdgcn_readfirstlane(tid >> 6);
  const int r = lane >> 4, c = lane & 15;
  const int bid = blockIdx.x, xcd = bid & 7, idx = bid >> 3;
  const int h = xcd * 4 + (idx >> 3), b = (idx >> 2) & 1, sp = idx & 3;
  const bool is_fin = idx >= 28;
  const int f = xcd * 4 + (idx - 28), fb = f >> 4, cg = f & 15;
  const int j = xcd * 28 + idx;
  constexpr float C2 = 0.088388347648318440f * 1.4426950408889634f;

  const __amdgpu_buffer_rsrc_t rs_ws = __builtin_amdgcn_make_buffer_rsrc((void*)ws, 0, WS_BYTES, RSRC_W3);
  const size_t kv_off = ((size_t)(b * NH + h) * SK + (size_t)sp * (SK / 4)) * DH;
  const __amdgpu_buffer_rsrc_t rs_k = __builtin_amdgcn_make_buffer_rsrc((void*)(kc + kv_off), 0, (SK / 4) * DH * 2, RSRC_W3);
  const __amdgpu_buffer_rsrc_t rs_v = __builtin_amdgcn_make_buffer_rsrc((void*)(vc + kv_off), 0, (SK / 4) * DH * 2, RSRC_W3);
  const __amdgpu_buffer_rsrc_t rs_wq = __builtin_amdgcn_make_buffer_rsrc((void*)wq, 0, DM * NH * DH * 2, RSRC_W3);
  const __amdgpu_buffer_rsrc_t rs_wo = __builtin_amdgcn_make_buffer_rsrc((void*)wo, 0, DM * NH * DH * 2, RSRC_W3);
  const __amdgpu_buffer_rsrc_t rs_w1 = __builtin_amdgcn_make_buffer_rsrc((void*)w1, 0, DM * FF * 2, RSRC_W3);
  const __amdgpu_buffer_rsrc_t rs_w2 = __builtin_amdgcn_make_buffer_rsrc((void*)w2, 0, DM * FF * 2, RSRC_W3);
  const int fin_base = WS_SLOT + f * FIN_BYTES;

  if (is_fin) {
    const v4u s4 = {SENT, SENT, SENT, SENT};
#pragma unroll
    for (int k = 0; k < 6; ++k) {
      const int off = tid * 16 + k * 8192;
      if (off < FIN_BYTES) __builtin_amdgcn_raw_buffer_store_b128(s4, rs_ws, fin_base + off, 0, AUX_SC1);
    }
  }
  SCHED_FENCE();
  const u64 tag = __hip_atomic_load((GAS u64*)ws, RLX_AGENT) + 1ull;
  const unsigned x0u = x[tid], x1u = x[DM + tid];
  unsigned gwu = gw[tid];
  const v4u w1v = __builtin_amdgcn_raw_buffer_load_b128(rs_w1, is_fin ? OOB : tid * (FF * 2) + j * 16, 0, 0);
  v2u w2v[8];
  {
    const int voff = (is_fin || w >= 4) ? OOB : (tid & 127) * 8 + j * 8192;
#pragma unroll
    for (int i = 0; i < 8; ++i) w2v[i] = __builtin_amdgcn_raw_buffer_load_b64(rs_w2, voff + (i & 3) * 1024, (i >> 2) * 4096, 0);
  }
  v4u wreg[16];
  {
    const int voff = (64 * w + 16 * r) * (NH * DH * 2) + h * (DH * 2) + c * 16;
#pragma unroll
    for (int i = 0; i < 16; ++i) wreg[i] = __builtin_amdgcn_raw_buffer_load_b128(rs_wq, voff, i * (NH * DH * 2), 0);
  }
  SCHED_FENCE();
  const int kv_voff = lane * 16 + w * 8192;
  v4u ring[RING][8];
#pragma unroll
  for (int g = 0; g < RING; ++g) issue_group((g & 1) ? rs_v : rs_k, kv_voff, (g >> 1) * 65536, ring[g]);
  SCHED_FENCE();

  const float x0 = bf_lo(x0u), x1 = bf_lo(x1u);
  {
    float pv[16];
#pragma unroll
    for (int k = 0; k < 4; ++k) {
      pv[2 * k] = x0 * bf_lo(w1v[k]);
      pv[2 * k + 1] = x0 * bf_hi(w1v[k]);
      pv[8 + 2 * k] = x1 * bf_lo(w1v[k]);
      pv[8 + 2 * k + 1] = x1 * bf_hi(w1v[k]);
    }
    asm volatile("" : "+v"(gwu));
    const float gp = round_bf16((fb ? x1 : x0) * bf_lo(gwu));
    pv[0] = is_fin ? gp : pv[0];
    float t = transpose_reduce16(pv, c);
    t = xrow_sum(t);
    if (r == 0) lds[L_HP + w * 16 + c] = t;
  }
  {
    float qa[8];
#pragma unroll
    for (int k = 0; k < 8; ++k) qa[k] = 0.f;
    BcastFma<0, 16, 1>::run(b ? x1 : x0, wreg, qa);
#pragma unroll
    for (int k = 0; k < 8; ++k) qa[k] = xrow_sum(qa[k]);
    if (r == 0) {
      *(v4f*)&lds[L_QP + w * 128 + 8 * c] = v4f{qa[0], qa[1], qa[2], qa[3]};
      *(v4f*)&lds[L_QP + w * 128 + 8 * c + 4] = v4f{qa[4], qa[5], qa[6], qa[7]};
    }
  }
  wg_barrier();
  if (tid < 64) {
    float s0 = 0.f, s1 = 0.f;
#pragma unroll
    for (int ww = 0; ww < 8; ++ww) {
      const v2f t = *(const v2f*)&lds[L_QP + ww * 128 + 2 * tid];
      s0 += t.x;
      s1 += t.y;
    }
    ((unsigned*)lds)[L_Q16 + tid] = pack_bf16(s0, s1);
  } else if (tid < 80) {
    float s = 0.f;
#pragma unroll
    for (int ww = 0; ww < 8; ++ww) s += lds[L_HP + ww * 16 + (tid - 64)];
    s = round_bf16(s);
    const float sg = 1.f / (1.f + __expf(-s));
    lds[L_HS + (tid - 64)] = round_bf16(is_fin ? sg : s * sg);
  }
  if (is_fin && tid == 128) __hip_atomic_store((GAS u64*)(ws + WS_READY) + f, tag, RLX_AGENT);
  wg_barrier();
  const v4u qv = *(const v4u*)&lds[L_Q16 + 4 * c];
  v4f z4 = {0.f, 0.f, 0.f, 0.f};
  {
    const int zb = (w >> 1) & 1;
#pragma unroll
    for (int i = 0; i < 8; ++i) {
      const float hv = lds[L_HS + zb * 8 + i];
      z4[0] = fmaf(hv, bf_lo(w2v[i][0]), z4[0]);
      z4[1] = fmaf(hv, bf_hi(w2v[i][0]), z4[1]);
      z4[2] = fmaf(hv, bf_lo(w2v[i][1]), z4[2]);
      z4[3] = fmaf(hv, bf_hi(w2v[i][1]), z4[3]);
    }
    PIN4(z4);
  }
  SCHED_FENCE();
  {
    const int voff = lane * 16;
    const int soff = (h * DH + 16 * w) * (DM * 2);
#pragma unroll
    for (int i = 0; i < 16; ++i)
      wreg[i] = __builtin_amdgcn_raw_buffer_load_b128(rs_wo, voff + (i & 3) * 1024, soff + (i >> 2) * 4096, 0);
  }
  SCHED_FENCE();

  float m = -INFINITY, l = 0.f, p = 0.f, acc[8];
#pragma unroll
  for (int k = 0; k < 8; ++k) acc[k] = 0.f;
  u64 rdy = 0;
  v4u zr[4];
  unsigned wp[64];
#pragma unroll
  for (int g = 0; g < NGRP; ++g) {
    v4u(&cur)[8] = ring[g % RING];
    float s[8];
    if ((g & 1) == 0) {
#pragma unroll
      for (int i = 0; i < 8; ++i) {
        float d = dot2(cur[i][0], qv[0], 0.f);
        d = dot2(cur[i][1], qv[1], d);
        d = dot2(cur[i][2], qv[2], d);
        s[i] = dot2(cur[i][3], qv[3], d);
      }
      PIN8(s);
    } else {
      BcastFma<0, 8, 2>::run(p, cur, acc);
    }
    SCHED_FENCE();
    if (g == 6) rdy = __hip_atomic_load((GAS u64*)(ws + WS_READY) + (lane & 31), RLX_AGENT);
    if (g + RING < NGRP) {
      issue_group(((g + RING) & 1) ? rs_v : rs_k, kv_voff, ((g + RING) >> 1) * 65536, cur);
    } else if (g + RING == NGRP) {
      const int voff = is_fin ? fin_base + Y_BYTES + tid * 16 : OOB;
#pragma unroll
      for (int q = 0; q < 4; ++q) zr[q] = __builtin_amdgcn_raw_buffer_load_b128(rs_ws, voff, q * 8192, AUX_SC1);
    }
    SCHED_FENCE();
    if (g == 4) {
#pragma unroll
      for (int pp = 0; pp < 8; ++pp) {
#pragma unroll
        for (int d = 0; d < 4; ++d) {
          const unsigned a0 = wreg[2 * pp][d], a1 = wreg[2 * pp + 1][d];
          wp[8 * pp + 2 * d] = __builtin_amdgcn_perm(a1, a0, 0x05040100u);
          wp[8 * pp + 2 * d + 1] = __builtin_amdgcn_perm(a1, a0, 0x07060302u);
        }
        PIN8(wp + 8 * pp);
        SCHED_FENCE();
      }
    }
    if (g == 6 + RING) {
      if (__builtin_expect(!__all(rdy == tag), 0)) {
        unsigned spins = 0;
        do {
          __builtin_amdgcn_s_sleep(2);
          rdy = __hip_atomic_load((GAS u64*)(ws + WS_READY) + (lane & 31), RLX_AGENT);
        } while (!__all(rdy == tag) && ++spins < SPIN_CAP);
      }
      {
        const int zb = (w >> 1) & 1, col4 = tid & 127;
        const v4u o = {canon(z4[0]), canon(z4[1]), canon(z4[2]), canon(z4[3])};
        const int voff = WS_SLOT + (zb * 16 + (col4 >> 3)) * FIN_BYTES + Y_BYTES + j * 128 + (col4 & 7) * 16;
        __builtin_amdgcn_raw_buffer_store_b128(o, rs_ws, (is_fin || w >= 4) ? OOB : voff, 0, AUX_SC1);
      }
    }
    if ((g & 1) == 0) {
      const float sc = transpose_reduce8(s, c) * C2;
      float pm = fmaxf(sc, dpp<XOR8>(sc));
      pm = fmaxf(pm, dpp<XOR7>(pm));
      pm = fmaxf(pm, dpp<XOR2>(pm));
      pm = xrow_max(pm);
      const float mn = fmaxf(m, pm);
      p = __builtin_amdgcn_exp2f(sc - mn);
      if (g > 0) {
        const float alpha = __builtin_amdgcn_exp2f(m - mn);
        l *= alpha;
#pragma unroll
        for (int k = 0; k < 8; ++k) acc[k] *= alpha;
      }
      l += p;
      m = mn;
    }
  }

#pragma unroll
  for (int k = 0; k < 8; ++k) acc[k] = xrow_sum(acc[k]);
  l = 0.5f * xrow_sum(row_sum(l));
  if (r == 0) {
    *(v4f*)&lds[L_O + w * 128 + 8 * c] = v4f{acc[0], acc[1], acc[2], acc[3]};
    *(v4f*)&lds[L_O + w * 128 + 8 * c + 4] = v4f{acc[4], acc[5], acc[6], acc[7]};
    if (c == 0) {
      lds[L_M + w] = m;
      lds[L_L + w] = l;
    }
  }
  wg_barrier();
  const int row = h * 4 + sp;
  {
    const int sw = lane >> 3, pr = lane & 7;
    const v4f ma = *(const v4f*)&lds[L_M], mb = *(const v4f*)&lds[L_M + 4];
    const float msw = lds[L_M + sw], lsw = lds[L_L + sw];
    const v2f o2 = *(const v2f*)&lds[L_O + sw * 128 + 16 * w + 2 * pr];
    const float M = fmaxf(fmaxf(fmaxf(ma[0], ma[1]), fmaxf(ma[2], ma[3])), fmaxf(fmaxf(mb[0], mb[1]), fmaxf(mb[2], mb[3])));
    const float e = __builtin_amdgcn_exp2f(msw - M);
    float oa = o2.x * e, ob = o2.y * e;
    oa += dpp<XOR8>(oa);
    ob += dpp<XOR8>(ob);
    oa = xrow_sum(oa);
    ob = xrow_sum(ob);
    if (w == 7) {
      float L = lsw * e;
      L += dpp<XOR8>(L);
      L = xrow_sum(L);
      if (lane < 16) {
        const v2u o = {canon(M), canon(L)};
        __builtin_amdgcn_raw_buffer_store_b64(o, rs_ws, WS_SLOT + (b * 16 + lane) * FIN_BYTES + Y_BYTES + Z_BYTES + row * 8, 0, AUX_SC1);
      }
    }
    const unsigned opk = pack_bf16(oa, ob);
    float y[8];
#pragma unroll
    for (int k = 0; k < 8; ++k) y[k] = 0.f;
#pragma unroll
    for (int p = 0; p < 8; ++p) {
      const unsigned so = __builtin_amdgcn_readlane(opk, p);
#pragma unroll
      for (int d = 0; d < 8; ++d) y[d] = dot2(wp[8 * p + d], so, y[d]);
    }
    *(v4f*)&lds[L_Y + w * 512 + lane * 4] = v4f{y[0], y[1], y[2], y[3]};
    *(v4f*)&lds[L_Y + w * 512 + 256 + lane * 4] = v4f{y[4], y[5], y[6], y[7]};
  }
  wg_barrier();
  if (tid < 128) {
    v4f y4 = {0.f, 0.f, 0.f, 0.f};
#pragma unroll
    for (int ww = 0; ww < 8; ++ww) y4 += *(const v4f*)&lds[L_Y + ww * 512 + (tid & 1) * 256 + (tid >> 1) * 4];
    const v4u o = {canon(y4[0]), canon(y4[1]), canon(y4[2]), canon(y4[3])};
    __builtin_amdgcn_raw_buffer_store_b128(o, rs_ws, WS_SLOT + (b * 16 + (tid >> 3)) * FIN_BYTES + row * 128 + (tid & 7) * 16, 0, AUX_SC1);
  }
  if (!is_fin) return;

  {
    unsigned spins = 0;
    for (;;) {
      bool ok = true;
#pragma unroll
      for (int q = 0; q < 4; ++q) {
        const bool live = q * 64 + (tid >> 3) < NZ;
        ok = ok && (!live || (zr[q][0] != SENT && zr[q][1] != SENT && zr[q][2] != SENT && zr[q][3] != SENT));
      }
      if (__all(ok) || ++spins > SPIN_CAP) break;
      __builtin_amdgcn_s_sleep(2);
      asm volatile("" ::: "memory");
#pragma unroll
      for (int q = 0; q < 4; ++q)
        zr[q] = __builtin_amdgcn_raw_buffer_load_b128(rs_ws, fin_base + Y_BYTES + tid * 16, q * 8192, AUX_SC1);
    }
    z4 = v4f{0.f, 0.f, 0.f, 0.f};
#pragma unroll
    for (int q = 0; q < 4; ++q)
      if (q * 64 + (tid >> 3) < NZ) z4 += __builtin_bit_cast(v4f, zr[q]);
    v4u yv[2], mlv[2][2];
    spins = 0;
    for (;;) {
#pragma unroll
      for (int p = 0; p < 2; ++p) {
        yv[p] = __builtin_amdgcn_raw_buffer_load_b128(rs_ws, fin_base + p * 8192 + tid * 16, 0, AUX_SC1);
        const int mo = fin_base + Y_BYTES + Z_BYTES + p * 512 + (tid >> 5) * 32;
        mlv[p][0] = __builtin_amdgcn_raw_buffer_load_b128(rs_ws, mo, 0, AUX_SC1);
        mlv[p][1] = __builtin_amdgcn_raw_buffer_load_b128(rs_ws, mo + 16, 0, AUX_SC1);
      }
      bool ok = true;
#pragma unroll
      for (int p = 0; p < 2; ++p)
#pragma unroll
        for (int k = 0; k < 4; ++k) ok = ok && yv[p][k] != SENT && mlv[p][0][k] != SENT && mlv[p][1][k] != SENT;
      if (__all(ok)) break;
      if (++spins > SPIN_CAP) break;
      __builtin_amdgcn_s_sleep(1);
      asm volatile("" ::: "memory");
    }
    const float g = lds[L_HS];
    v4f a4 = {0.f, 0.f, 0.f, 0.f};
    const int sown = (tid >> 3) & 3;
#pragma unroll
    for (int p = 0; p < 2; ++p) {
      const float m0 = __uint_as_float(mlv[p][0][0]), l0 = __uint_as_float(mlv[p][0][1]);
      const float m1 = __uint_as_float(mlv[p][0][2]), l1 = __uint_as_float(mlv[p][0][3]);
      const float m2 = __uint_as_float(mlv[p][1][0]), l2 = __uint_as_float(mlv[p][1][1]);
      const float m3 = __uint_as_float(mlv[p][1][2]), l3 = __uint_as_float(mlv[p][1][3]);
      const float M = fmaxf(fmaxf(m0, m1), fmaxf(m2, m3));
      const float e0 = __builtin_amdgcn_exp2f(m0 - M), e1 = __builtin_amdgcn_exp2f(m1 - M);
      const float e2 = __builtin_amdgcn_exp2f(m2 - M), e3 = __builtin_amdgcn_exp2f(m3 - M);
      const float L = (l0 * e0 + l1 * e1) + (l2 * e2 + l3 * e3);
      const float eo = sown == 0 ? e0 : sown == 1 ? e1 : sown == 2 ? e2 : e3;
      const float wgt = eo / L;
      a4 += wgt * __builtin_bit_cast(v4f, yv[p]);
    }
    const float g1 = round_bf16(1.f - g);
    v4f t4 = g * a4 + g1 * z4;
#pragma unroll
    for (int k = 0; k < 4; ++k) {
      float t = t4[k];
      t += dpp<XOR8>(t);
      t4[k] = xrow_sum(t);
    }
    if (lane < 8) *(v4f*)&lds[L_FIN + w * 32 + lane * 4] = t4;
    wg_barrier();
    if (tid < 16) {
      float s0 = 0.f, s1 = 0.f;
#pragma unroll
      for (int ww = 0; ww < 8; ++ww) {
        const v2f t = *(const v2f*)&lds[L_FIN + ww * 32 + 2 * tid];
        s0 += t.x;
        s1 += t.y;
      }
      ((unsigned*)out)[fb * (DM / 2) + cg * 16 + tid] = pack_bf16(s0, s1);
    }
    if (f == 0 && tid == 64) __hip_atomic_store((GAS u64*)ws, tag, RLX_AGENT);
  }
}

extern "C" void kernel_launch(void* const* d_in, const int* in_sizes, int n_in, void* d_out, int out_size, void* d_ws,
                              size_t ws_size, hipStream_t stream) {
  (void)in_sizes; (void)n_in; (void)out_size; (void)ws_size;
  fused_decode_kernel<<<NWG, NTHR, 0, stream>>>((const u16*)d_in[0], (const u16*)d_in[1], (const u16*)d_in[2],
                                                (const u16*)d_in[3], (const u16*)d_in[4], (const u16*)d_in[6],
                                                (const u16*)d_in[7], (const u16*)d_in[8], (u16*)d_out,
                                                (unsigned char*)d_ws);
}
```

```cpp
#include <hip/hip_runtime.h>
#include <stdint.h>

typedef unsigned short u16;
typedef unsigned long long u64;
typedef unsigned v4u __attribute__((ext_vector_type(4)));
typedef int v4i __attribute__((ext_vector_type(4)));
typedef float v4f __attribute__((ext_vector_type(4)));
typedef float v2f __attribute__((ext_vector_type(2)));
typedef __bf16 bf16x2 __attribute__((ext_vector_type(2)));
#define GAS __attribute__((address_space(1)))
#define LAS __attribute__((address_space(3)))
#define RLX_AGENT __ATOMIC_RELAXED, __HIP_MEMORY_SCOPE_AGENT


constexpr int PRO = 3;
constexpr int PACE = 2;
constexpr int SKEWT = 8;
constexpr int DM = 512, NH = 32, SK = 8192, DH = 128, FF = 1792;
constexpr int NWG = 256, NTHR = 576;
constexpr int NTW = 16;
constexpr int NFIN = 32, HU = FF / NH;
constexpr int WS_QP = 4096;
constexpr int WS_HP = WS_QP + NWG * 2048;
constexpr int WS_OP = WS_HP + NWG * 1024;
constexpr int OP_BYTES = 1152;
constexpr int WS_Y = WS_OP + NWG * OP_BYTES;
constexpr int WS_BYTES = WS_Y + NFIN * NH * 256;
constexpr int AUX_SC1 = 16, AUX_NT = 2;
constexpr int RSRC_W3 = 0x00020000;
constexpr unsigned SPIN_CAP = 1u << 15;
constexpr int OOB = 0x7FFF0000;

__device__ __forceinline__ float bf_lo(unsigned u) { return __builtin_bit_cast(float, u << 16); }
__device__ __forceinline__ float bf_hi(unsigned u) { return __builtin_bit_cast(float, u & 0xffff0000u); }
__device__ __forceinline__ float dot2(unsigned a, unsigned b, float c) {
  return __builtin_amdgcn_fdot2_f32_bf16(__builtin_bit_cast(bf16x2, a), __builtin_bit_cast(bf16x2, b), c, false);
}
__device__ __forceinline__ unsigned pack_bf16(float lo, float hi) {
  bf16x2 v;
  v.x = (__bf16)lo;
  v.y = (__bf16)hi;
  return __builtin_bit_cast(unsigned, v);
}
__device__ __forceinline__ float round_bf16(float f) { return bf_lo(pack_bf16(f, 0.f) & 0xffffu); }
template <int CTRL> __device__ __forceinline__ float dpp(float x) {
  return __builtin_bit_cast(float, __builtin_amdgcn_mov_dpp(__builtin_bit_cast(int, x), CTRL, 0xf, 0xf, true));
}
constexpr int XOR1 = 0xB1, XOR2 = 0x4E, XOR7 = 0x141, XOR8 = 0x128, BCAST = 0x150;
__device__ __forceinline__ float x16_max(float x) {
  auto s = __builtin_amdgcn_permlane16_swap(__float_as_uint(x), __float_as_uint(x), false, false);
  return fmaxf(__uint_as_float(s[0]), __uint_as_float(s[1]));
}
__device__ __forceinline__ float x16_sum(float x) {
  auto s = __builtin_amdgcn_permlane16_swap(__float_as_uint(x), __float_as_uint(x), false, false);
  return __uint_as_float(s[0]) + __uint_as_float(s[1]);
}
__device__ __forceinline__ float xrow_max(float x) {
  x = x16_max(x);
  auto t = __builtin_amdgcn_permlane32_swap(__float_as_uint(x), __float_as_uint(x), false, false);
  return fmaxf(__uint_as_float(t[0]), __uint_as_float(t[1]));
}
__device__ __forceinline__ float xrow_sum(float x) {
  x = x16_sum(x);
  auto t = __builtin_amdgcn_permlane32_swap(__float_as_uint(x), __float_as_uint(x), false, false);
  return __uint_as_float(t[0]) + __uint_as_float(t[1]);
}
__device__ __forceinline__ float row_sum(float x) {
  x += dpp<XOR8>(x);
  x += dpp<XOR7>(x);
  x += dpp<XOR2>(x);
  x += dpp<XOR1>(x);
  return x;
}
__device__ __forceinline__ float transpose_reduce4(float (&x)[4], int c) {
  const float a0 = x[0] + dpp<XOR8>(x[0]), a1 = x[1] + dpp<XOR8>(x[1]);
  const float a2 = x[2] + dpp<XOR8>(x[2]), a3 = x[3] + dpp<XOR8>(x[3]);
  const float b0 = (c & 8) ? a2 : a0, b1 = (c & 8) ? a3 : a1;
  const float c0 = b0 + dpp<XOR7>(b0), c1 = b1 + dpp<XOR7>(b1);
  float d = (c & 4) ? c1 : c0;
  d += dpp<XOR2>(d);
  return d + dpp<XOR1>(d);
}
__device__ __forceinline__ void wg_barrier() { asm volatile("s_waitcnt lgkmcnt(0)\n\ts_barrier" ::: "memory"); }
#define SCHED_FENCE() __builtin_amdgcn_sched_barrier(0)
#define PIN4(v) asm volatile("" : "+v"((v)[0]), "+v"((v)[1]), "+v"((v)[2]), "+v"((v)[3]))
#define PIN8(v) asm volatile("" : "+v"((v)[0]), "+v"((v)[1]), "+v"((v)[2]), "+v"((v)[3]), "+v"((v)[4]), "+v"((v)[5]), "+v"((v)[6]), "+v"((v)[7]))

template <int I> struct BcastFma {
  static __device__ __forceinline__ void run(float p, const v4u (&v)[4], float (&acc)[8]) {
    const float pb = dpp<BCAST + I * 4>(p);
#pragma unroll
    for (int k = 0; k < 4; ++k) {
      acc[2 * k] = fmaf(pb, bf_lo(v[I][k]), acc[2 * k]);
      acc[2 * k + 1] = fmaf(pb, bf_hi(v[I][k]), acc[2 * k + 1]);
    }
    PIN8(acc);
    BcastFma<I + 1>::run(p, v, acc);
  }
};
template <> struct BcastFma<4> {
  static __device__ __forceinline__ void run(float, const v4u (&)[4], float (&)[8]) {}
};
template <int P> struct PairDot {
  static __device__ __forceinline__ void run(unsigned xa, unsigned xb, const v4u (&v)[16], float (&aa)[8], float (&ab)[8]) {
    const unsigned pa = (unsigned)__builtin_amdgcn_mov_dpp((int)xa, BCAST + P, 0xf, 0xf, true);
    const unsigned pb = (unsigned)__builtin_amdgcn_mov_dpp((int)xb, BCAST + P, 0xf, 0xf, true);
#pragma unroll
    for (int k = 0; k < 4; ++k) {
      const unsigned lo = __builtin_amdgcn_perm(v[2 * P + 1][k], v[2 * P][k], 0x05040100u);
      const unsigned hi = __builtin_amdgcn_perm(v[2 * P + 1][k], v[2 * P][k], 0x07060302u);
      aa[2 * k] = dot2(lo, pa, aa[2 * k]);
      aa[2 * k + 1] = dot2(hi, pa, aa[2 * k + 1]);
      ab[2 * k] = dot2(lo, pb, ab[2 * k]);
      ab[2 * k + 1] = dot2(hi, pb, ab[2 * k + 1]);
    }
    PIN8(aa);
    PIN8(ab);
    PairDot<P + 1>::run(xa, xb, v, aa, ab);
  }
};
template <> struct PairDot<8> {
  static __device__ __forceinline__ void run(unsigned, unsigned, const v4u (&)[16], float (&)[8], float (&)[8]) {}
};
__device__ __forceinline__ void issue_group(__amdgpu_buffer_rsrc_t rs, int voff, v4u (&dst)[4]) {
#pragma unroll
  for (int i = 0; i < 4; ++i) {
    dst[i] = __builtin_amdgcn_raw_buffer_load_b128(rs, voff + i * 1024, 0, AUX_NT);
    SCHED_FENCE();
  }
}

template <int STRIDE>
__device__ __forceinline__ void gather8(__amdgpu_buffer_rsrc_t rs, int voff, unsigned tagA, unsigned tagB, float& s0, float& s1) {
  unsigned spins = 0;
  for (;;) {
    v4u g[8];
#pragma unroll
    for (int i = 0; i < 8; ++i) g[i] = __builtin_amdgcn_raw_buffer_load_b128(rs, voff, i * STRIDE, AUX_SC1);
    unsigned bad = 0u;
    s0 = s1 = 0.f;
#pragma unroll
    for (int i = 0; i < 8; ++i) {
      bad |= (g[i][1] ^ tagA) | (g[i][3] ^ tagB);
      s0 += __uint_as_float(g[i][0]);
      s1 += __uint_as_float(g[i][2]);
    }
    if (!__any(bad != 0u) || ++spins > SPIN_CAP) break;
    asm volatile("" ::: "memory");
  }
}

constexpr int L_WP = 0;
constexpr int L_O = L_WP + 4096;
constexpr int L_YP = L_O + 8 * 128;
constexpr int L_Q16 = L_YP + 8 * 128;
constexpr int L_M = L_Q16 + 64;
constexpr int L_L = L_M + 8;
constexpr int L_ZS = L_L + 8;
constexpr int L_HS = L_ZS + 128;
constexpr int L_G = L_HS + 112;
constexpr int L_TAG = L_G + 2;
constexpr int L_QRDY = L_TAG + 3;
constexpr int L_SIDE = L_QRDY + 1;
constexpr int L_TOTAL = L_SIDE + 1;

__global__ void __launch_bounds__(NTHR, 3)
fused_decode_kernel(const u16* __restrict__ x, const u16* __restrict__ kc, const u16* __restrict__ vc,
                    const u16* __restrict__ wq, const u16* __restrict__ wo, const u16* __restrict__ w1,
                    const u16* __restrict__ w2, const u16* __restrict__ gw, u16* __restrict__ out,
                    unsigned char* ws) {
  __shared__ __attribute__((aligned(16))) float lds[L_TOTAL];
  volatile LAS unsigned* ldsu = (volatile LAS unsigned*)lds;
  asm volatile("" ::"s"(x), "s"(kc), "s"(vc), "s"(wq), "s"(wo), "s"(w1), "s"(w2), "s"(gw), "s"(out), "s"(ws));
  const int tid = threadIdx.x, lane = tid & 63, w = __builtin_amdgcn_readfirstlane(tid >> 6);
  const int r = lane >> 4, c = lane & 15, lane16 = lane * 16;
  const int bid = blockIdx.x, h = bid >> 3, mm = bid & 7, b = mm >> 2, sp = mm & 3;
  const int cs = (mm + h) & 7;
  const bool is_fin = h >= 28;
  const int f = mm * 4 + (h - 28), fb = f >> 4, cg = f & 15;
  const __amdgpu_buffer_rsrc_t rs_ws = __builtin_amdgcn_make_buffer_rsrc((void*)ws, 0, WS_BYTES, RSRC_W3);

  if (w == 0) {
    __builtin_amdgcn_s_setprio(3);
    const __amdgpu_buffer_rsrc_t rs_wq = __builtin_amdgcn_make_buffer_rsrc((void*)wq, 0, DM * NH * DH * 2, RSRC_W3);
    const __amdgpu_buffer_rsrc_t rs_wo = __builtin_amdgcn_make_buffer_rsrc((void*)wo, 0, DM * NH * DH * 2, RSRC_W3);
    const __amdgpu_buffer_rsrc_t rs_w1 = __builtin_amdgcn_make_buffer_rsrc((void*)w1, 0, DM * FF * 2, RSRC_W3);
    const __amdgpu_buffer_rsrc_t rs_w2 = __builtin_amdgcn_make_buffer_rsrc((void*)w2, 0, DM * FF * 2, RSRC_W3);
    v4u wr[16];
    {
      const int voff = (64 * mm + 16 * r) * (NH * DH * 2) + h * (DH * 2) + c * 16;
#pragma unroll
      for (int i = 0; i < 16; ++i) wr[i] = __builtin_amdgcn_raw_buffer_load_b128(rs_wq, voff, i * (NH * DH * 2), AUX_NT);
    }
    unsigned xq0 = ((const unsigned*)x)[32 * mm + 8 * r + (c & 7)], xq1 = ((const unsigned*)x)[DM / 2 + 32 * mm + 8 * r + (c & 7)];
    u64 epoch = __hip_atomic_load((GAS u64*)ws, RLX_AGENT);
    const int rr = lane >> 3, cc = lane & 7;
    v4u wf[8];
    v4u xa = *(const v4u*)(x + 64 * mm + 8 * rr), xb = *(const v4u*)(x + DM + 64 * mm + 8 * rr);
    {
      const int voff = cc < 7 ? (64 * mm + 8 * rr) * (FF * 2) + h * (HU * 2) + cc * 16 : OOB;
#pragma unroll
      for (int t = 0; t < 8; ++t) wf[t] = __builtin_amdgcn_raw_buffer_load_b128(rs_w1, voff, t * (FF * 2), AUX_NT);
    }
    if (lane < 2) ldsu[L_QRDY + lane] = 0u;
    SCHED_FENCE();
    wg_barrier();
    asm volatile("" : "+v"(xq0), "+v"(xq1));
    SCHED_FENCE();
    unsigned tagA, tagB;
#define FOLD54(a_, b_, t4_)                                                                                              \
  {                                                                                                                      \
    float t8_[8];                                                                                                        \
    _Pragma("unroll") for (int k = 0; k < 8; ++k) {                                                                      \
      auto s_ = __builtin_amdgcn_permlane32_swap(__float_as_uint((a_)[k]), __float_as_uint((b_)[k]), false, false);      \
      t8_[k] = __uint_as_float(s_[0]) + __uint_as_float(s_[1]);                                                          \
    }                                                                                                                    \
    _Pragma("unroll") for (int k = 0; k < 4; ++k) {                                                                      \
      auto s_ = __builtin_amdgcn_permlane16_swap(__float_as_uint(t8_[k]), __float_as_uint(t8_[k + 4]), false, false);    \
      (t4_)[k] = __uint_as_float(s_[0]) + __uint_as_float(s_[1]);                                                        \
    }                                                                                                                    \
  }
#define FOLD3(t4_, o0_, o1_)                                                                                             \
  {                                                                                                                      \
    const float u0_ = (t4_)[0] + dpp<XOR8>((t4_)[0]), u1_ = (t4_)[1] + dpp<XOR8>((t4_)[1]);                              \
    const float u2_ = (t4_)[2] + dpp<XOR8>((t4_)[2]), u3_ = (t4_)[3] + dpp<XOR8>((t4_)[3]);                              \
    o0_ = (lane & 8) ? u2_ : u0_;                                                                                        \
    o1_ = (lane & 8) ? u3_ : u1_;                                                                                        \
  }
    {
      float qa[8], qb[8], t4[4];
#pragma unroll
      for (int k = 0; k < 8; ++k) qa[k] = qb[k] = 0.f;
      PairDot<0>::run(xq0, xq1, wr, qa, qb);
      asm volatile("" : "+v"(epoch));
      epoch += 1ull;
      tagA = (unsigned)epoch;
      tagB = (unsigned)(epoch >> 32) + tagA;
      FOLD54(qa, qb, t4)
      __builtin_amdgcn_raw_buffer_store_b128(v4u{__float_as_uint(t4[0]), tagA, __float_as_uint(t4[1]), tagB}, rs_ws,
                                             WS_QP + bid * 2048 + lane16, 0, AUX_SC1);
      __builtin_amdgcn_raw_buffer_store_b128(v4u{__float_as_uint(t4[2]), tagA, __float_as_uint(t4[3]), tagB}, rs_ws,
                                             WS_QP + bid * 2048 + lane16, 1024, AUX_SC1);
    }
    {
      float ha[8], hb[8], t4[4], o0, o1;
#pragma unroll
      for (int k = 0; k < 8; ++k) ha[k] = hb[k] = 0.f;
#pragma unroll
      for (int t = 0; t < 4; ++t) {
#pragma unroll
        for (int k = 0; k < 4; ++k) {
          const unsigned lo = __builtin_amdgcn_perm(wf[2 * t + 1][k], wf[2 * t][k], 0x05040100u);
          const unsigned hi = __builtin_amdgcn_perm(wf[2 * t + 1][k], wf[2 * t][k], 0x07060302u);
          ha[2 * k] = dot2(lo, xa[t], ha[2 * k]);
          ha[2 * k + 1] = dot2(hi, xa[t], ha[2 * k + 1]);
          hb[2 * k] = dot2(lo, xb[t], hb[2 * k]);
          hb[2 * k + 1] = dot2(hi, xb[t], hb[2 * k + 1]);
        }
      }
      FOLD54(ha, hb, t4)
      FOLD3(t4, o0, o1)
      __builtin_amdgcn_raw_buffer_store_b128(v4u{__float_as_uint(o0), tagA, __float_as_uint(o1), tagB}, rs_ws,
                                             WS_HP + bid * 1024 + lane16, 0, AUX_SC1);
    }
    {
      float s0, s1;
      gather8<2048>(rs_ws, WS_QP + h * 16384 + (lane >> 5) * 1024 + b * 512 + (lane & 31) * 16, tagA, tagB, s0, s1);
      ldsu[L_Q16 + 4 * c + 2 * (r & 1) + (r >> 1)] = pack_bf16(s0, s1);
      if (lane == 0) {
        ldsu[L_TAG] = tagA;
        ldsu[L_TAG + 1] = tagB;
        ldsu[L_TAG + 2] = (unsigned)(epoch >> 32);
      }
      asm volatile("s_waitcnt lgkmcnt(0)" ::: "memory");
      if (lane == 0) ldsu[L_QRDY] = 1u;
    }
    __builtin_amdgcn_s_setprio(0);
    {
      const int v2 = (h * HU + rr) * (DM * 2) + cs * 128 + cc * 16;
#pragma unroll
      for (int t = 0; t < 7; ++t) wf[t] = __builtin_amdgcn_raw_buffer_load_b128(rs_w2, v2, t * (8 * DM * 2), AUX_NT);
      const int vo = (h * DH + 2 * rr) * (DM * 2) + cs * 128 + cc * 16;
#pragma unroll
      for (int i = 0; i < 16; ++i)
        wr[i] = __builtin_amdgcn_raw_buffer_load_b128(rs_wo, vo + (i & 1) * (DM * 2), (i >> 1) * (16 * DM * 2), AUX_NT);
    }
    xa = *(const v4u*)(x + 8 * lane);
    xb = *(const v4u*)(x + DM + 8 * lane);
    const v4u gwv = *(const v4u*)(gw + 8 * lane);
    SCHED_FENCE();
    {
      float s0, s1;
      gather8<1024>(rs_ws, WS_HP + h * 8192 + lane16, tagA, tagB, s0, s1);
      s0 = round_bf16(s0);
      s1 = round_bf16(s1);
      s0 = round_bf16(s0 / (1.f + __expf(-s0)));
      s1 = round_bf16(s1 / (1.f + __expf(-s1)));
      if (cc < 7) *(v2f*)&lds[L_HS + (lane >> 5) * HU + 8 * cc + 4 * ((lane >> 4) & 1) + 2 * ((lane >> 3) & 1)] = v2f{s0, s1};
    }
    float g0, g1;
    {
      float p0 = 0.f, p1 = 0.f;
#pragma unroll
      for (int k = 0; k < 4; ++k) {
        p0 += round_bf16(bf_lo(xa[k]) * bf_lo(gwv[k])) + round_bf16(bf_hi(xa[k]) * bf_hi(gwv[k]));
        p1 += round_bf16(bf_lo(xb[k]) * bf_lo(gwv[k])) + round_bf16(bf_hi(xb[k]) * bf_hi(gwv[k]));
      }
      p0 = round_bf16(xrow_sum(row_sum(p0)));
      p1 = round_bf16(xrow_sum(row_sum(p1)));
      g0 = round_bf16(1.f / (1.f + __expf(-p0)));
      g1 = round_bf16(1.f / (1.f + __expf(-p1)));
    }
    {
      float za[8], zb[8], t4[4], o0, o1;
#pragma unroll
      for (int k = 0; k < 8; ++k) za[k] = zb[k] = 0.f;
#pragma unroll
      for (int t = 0; t < 7; ++t) {
        const float h0 = lds[L_HS + 8 * t + rr], h1 = lds[L_HS + HU + 8 * t + rr];
#pragma unroll
        for (int k = 0; k < 4; ++k) {
          const float lo = bf_lo(wf[t][k]), hi = bf_hi(wf[t][k]);
          za[2 * k] = fmaf(h0, lo, za[2 * k]);
          za[2 * k + 1] = fmaf(h0, hi, za[2 * k + 1]);
          zb[2 * k] = fmaf(h1, lo, zb[2 * k]);
          zb[2 * k + 1] = fmaf(h1, hi, zb[2 * k + 1]);
        }
      }
      FOLD54(za, zb, t4)
      FOLD3(t4, o0, o1)
      const float gc = round_bf16(1.f - ((lane >> 5) ? g1 : g0));
      *(v2f*)&lds[L_ZS + (lane >> 5) * 64 + 8 * cc + 4 * ((lane >> 4) & 1) + 2 * ((lane >> 3) & 1)] = v2f{gc * o0, gc * o1};
    }
#pragma unroll
    for (int t = 0; t < 8; ++t) {
      v4u lo_, hi_;
#pragma unroll
      for (int d = 0; d < 2; ++d) {
        lo_[2 * d] = __builtin_amdgcn_perm(wr[2 * t + 1][d], wr[2 * t][d], 0x05040100u);
        lo_[2 * d + 1] = __builtin_amdgcn_perm(wr[2 * t + 1][d], wr[2 * t][d], 0x07060302u);
        hi_[2 * d] = __builtin_amdgcn_perm(wr[2 * t + 1][2 + d], wr[2 * t][2 + d], 0x05040100u);
        hi_[2 * d + 1] = __builtin_amdgcn_perm(wr[2 * t + 1][2 + d], wr[2 * t][2 + d], 0x07060302u);
      }
      *(v4u*)&lds[L_WP + (8 * t + rr) * 64 + cc * 8] = lo_;
      *(v4u*)&lds[L_WP + (8 * t + rr) * 64 + cc * 8 + 4] = hi_;
    }
    asm volatile("s_waitcnt lgkmcnt(0)" ::: "memory");
    if (lane == 0) ldsu[L_SIDE] = 1u;
    wg_barrier();
    {
      const v4f ma = *(const v4f*)&lds[L_M], mb = *(const v4f*)&lds[L_M + 4];
      const v4f la = *(const v4f*)&lds[L_L], lb = *(const v4f*)&lds[L_L + 4];
      v2f ov[8];
#pragma unroll
      for (int i = 0; i < 8; ++i) ov[i] = *(const v2f*)&lds[L_O + i * 128 + 2 * lane];
      const float M = fmaxf(fmaxf(fmaxf(ma[0], ma[1]), fmaxf(ma[2], ma[3])), fmaxf(fmaxf(mb[0], mb[1]), fmaxf(mb[2], mb[3])));
      float L = 0.f, o0 = 0.f, o1 = 0.f;
#pragma unroll
      for (int i = 0; i < 8; ++i) {
        const float e = __builtin_amdgcn_exp2f((i < 4 ? ma[i & 3] : mb[i & 3]) - M);
        L = fmaf(i < 4 ? la[i & 3] : lb[i & 3], e, L);
        o0 = fmaf(ov[i].x, e, o0);
        o1 = fmaf(ov[i].y, e, o1);
      }
      __builtin_amdgcn_raw_buffer_store_b128(v4u{__float_as_uint(o0), tagA, __float_as_uint(o1), tagB}, rs_ws,
                                             WS_OP + bid * OP_BYTES + lane16, 0, AUX_SC1);
      __builtin_amdgcn_raw_buffer_store_b128(v4u{__float_as_uint(M), tagA, __float_as_uint(L), tagB}, rs_ws,
                                             lane == 0 ? WS_OP + bid * OP_BYTES + 1024 : OOB, 0, AUX_SC1);
    }
    const float gl = (lane >> 5) ? g1 : g0;
    const v2f zl = *(const v2f*)&lds[L_ZS + 2 * lane];
    const int yo = WS_Y + (((lane >> 5) * 16 + cs * 2 + ((lane >> 4) & 1)) * NH + h) * 256 + c * 16;
    wg_barrier();
    {
      float y0 = 0.f, y1 = 0.f;
#pragma unroll
      for (int i = 0; i < 8; ++i) {
        const v2f t = *(const v2f*)&lds[L_YP + i * 128 + 2 * lane];
        y0 += t.x;
        y1 += t.y;
      }
      y0 = fmaf(gl, y0, zl.x);
      y1 = fmaf(gl, y1, zl.y);
      __builtin_amdgcn_raw_buffer_store_b128(v4u{__float_as_uint(y0), tagA, __float_as_uint(y1), tagB}, rs_ws, yo, 0, AUX_SC1);
    }
    return;
  }

  const int sw = w - 1;
  constexpr float C2 = 0.088388347648318440f * 1.4426950408889634f;
  const int ntiles = (sp & 1) ? 8 * NTW - SKEWT : 8 * NTW + SKEWT;
  const int ntw = (ntiles - sw + 7) >> 3;
  const int key0 = (sp >> 1) * (SK / 2) + ((sp & 1) ? (8 * NTW + SKEWT) * 16 : 0);
  const int kv_bytes = ntiles * 16 * DH * 2;
  const size_t kv_off = ((size_t)(b * NH + h) * SK + (size_t)key0) * DH;
  const __amdgpu_buffer_rsrc_t rs_k = __builtin_amdgcn_make_buffer_rsrc((void*)(kc + kv_off), 0, kv_bytes, RSRC_W3);
  const __amdgpu_buffer_rsrc_t rs_v = __builtin_amdgcn_make_buffer_rsrc((void*)(vc + kv_off), 0, kv_bytes, RSRC_W3);
  v4u ka[PRO + 1][4], va[PRO + 1][4];
  float m = -INFINITY, l = 0.f, acc[8];
#pragma unroll
  for (int k = 0; k < 8; ++k) acc[k] = 0.f;
#define QK(n, s_)                                                \
  _Pragma("unroll") for (int i = 0; i < 4; ++i) {                \
    float d = dot2(ka[n][i][0], qv[0], 0.f);                     \
    d = dot2(ka[n][i][1], qv[1], d);                             \
    d = dot2(ka[n][i][2], qv[2], d);                             \
    (s_)[i] = dot2(ka[n][i][3], qv[3], d);                       \
  }                                                              \
  PIN4(s_)
#define SMPV(n, s_)                                              \
  {                                                              \
    const float sc = transpose_reduce4(s_, c) * C2;              \
    float pm = fmaxf(sc, dpp<XOR8>(sc));                         \
    pm = fmaxf(pm, dpp<XOR7>(pm));                               \
    pm = xrow_max(pm);                                           \
    const float mn = fmaxf(m, pm);                               \
    const float p = __builtin_amdgcn_exp2f(sc - mn);             \
    const float alpha = __builtin_amdgcn_exp2f(m - mn);          \
    l = fmaf(l, alpha, p);                                       \
    _Pragma("unroll") for (int k = 0; k < 8; ++k) acc[k] *= alpha; \
    m = mn;                                                      \
    BcastFma<0>::run(p, va[n], acc);                             \
  }
  const int voff0 = lane16 + sw * 4096;
  const int rot = (h + mm) % 15;
#define RND(k_) (((k_) + rot) >= ntw ? (k_) + rot - ntw : (k_) + rot)
  int kt = 0;
  {
    const v4i dk = {(int)(size_t)(kc + kv_off), (int)((size_t)(kc + kv_off) >> 32) & 0xffff, kv_bytes, RSRC_W3};
    const v4i dv = {(int)(size_t)(vc + kv_off), (int)((size_t)(vc + kv_off) >> 32) & 0xffff, kv_bytes, RSRC_W3};
    int stop = 0;
    asm volatile("s_barrier" ::: "memory");
#pragma unroll
    for (int n = 1; n <= PRO; ++n) {
      if (n > 1 && !stop) {
        asm volatile("s_waitcnt vmcnt(%0)" ::"i"(n == 2 ? 4 : PACE) : "memory");
        stop = __builtin_amdgcn_readfirstlane(ldsu[L_QRDY]);
      }
      const int voff = voff0 + RND(n - 1) * 32768;
      asm volatile(
          "s_cmp_lg_u32 %[stop], 0\n\t"
          "s_cbranch_scc1 .Lskip%=\n\t"
          "buffer_load_dwordx4 %0, %[vo], %[dk], 0 offen nt\n\t"
          "buffer_load_dwordx4 %1, %[vo], %[dk], 0 offen offset:1024 nt\n\t"
          "buffer_load_dwordx4 %2, %[vo], %[dk], 0 offen offset:2048 nt\n\t"
          "buffer_load_dwordx4 %3, %[vo], %[dk], 0 offen offset:3072 nt\n\t"
          "s_waitcnt vmcnt(%[pace])\n\t"
          "buffer_load_dwordx4 %4, %[vo], %[dv], 0 offen nt\n\t"
          "buffer_load_dwordx4 %5, %[vo], %[dv], 0 offen offset:1024 nt\n\t"
          "buffer_load_dwordx4 %6, %[vo], %[dv], 0 offen offset:2048 nt\n\t"
          "buffer_load_dwordx4 %7, %[vo], %[dv], 0 offen offset:3072 nt\n"
          ".Lskip%=:"
          : "=&v"(ka[n][0]), "=&v"(ka[n][1]), "=&v"(ka[n][2]), "=&v"(ka[n][3]), "=&v"(va[n][0]), "=&v"(va[n][1]),
            "=&v"(va[n][2]), "=&v"(va[n][3])
          : [stop] "s"(stop), [vo] "v"(voff), [dk] "s"(dk), [dv] "s"(dv), [pace] "i"(n == 1 ? 4 : PACE)
          : "scc", "memory");
      kt += 1 - stop;
    }
    SCHED_FENCE();
    if (!stop) asm volatile("s_waitcnt vmcnt(%0)" ::"i"(PACE) : "memory");
    issue_group(rs_k, voff0 + RND(kt) * 32768, ka[0]);
    issue_group(rs_v, voff0 + RND(kt) * 32768, va[0]);
    if (!stop) {
      unsigned spins = 0;
      while (ldsu[L_QRDY] == 0u && ++spins < (SPIN_CAP << 6)) __builtin_amdgcn_s_sleep(1);
      asm volatile("" ::: "memory");
    }
  }
  const v4u qv = *(const v4u*)&lds[L_Q16 + 4 * c];
  asm volatile("s_waitcnt vmcnt(8)" ::: "memory");
#pragma unroll
  for (int n = 1; n <= PRO; ++n) {
    asm volatile("" : "+v"(ka[n][0]), "+v"(ka[n][1]), "+v"(ka[n][2]), "+v"(ka[n][3]), "+v"(va[n][0]), "+v"(va[n][1]),
                      "+v"(va[n][2]), "+v"(va[n][3]));
    if (n <= kt) {
      float s_[4];
      QK(n, s_);
      SMPV(n, s_)
    }
    SCHED_FENCE();
  }
#pragma unroll 1
  for (++kt; kt < ntw; ++kt) {
    float s_[4];
    QK(0, s_);
    SCHED_FENCE();
    const int voff = voff0 + RND(kt) * 32768;
    issue_group(rs_k, voff, ka[0]);
    SMPV(0, s_)
    SCHED_FENCE();
    issue_group(rs_v, voff, va[0]);
  }
  unsigned wp[8], tagA, tagB;
  {
    float s_[4];
    QK(0, s_);
    SCHED_FENCE();
    {
      unsigned spins = 0;
      while (ldsu[L_SIDE] == 0u && ++spins < (SPIN_CAP << 6)) __builtin_amdgcn_s_sleep(1);
      asm volatile("" ::: "memory");
    }
#pragma unroll
    for (int p = 0; p < 8; ++p) wp[p] = __float_as_uint(lds[L_WP + (8 * sw + p) * 64 + lane]);
    tagA = __float_as_uint(lds[L_TAG]);
    tagB = __float_as_uint(lds[L_TAG + 1]);
    SCHED_FENCE();
    SMPV(0, s_)
  }

#pragma unroll
  for (int k = 0; k < 8; ++k) acc[k] = xrow_sum(acc[k]);
  l = 0.25f * xrow_sum(row_sum(l));
  if (r == 0) {
    *(v4f*)&lds[L_O + sw * 128 + 8 * c] = v4f{acc[0], acc[1], acc[2], acc[3]};
    *(v4f*)&lds[L_O + sw * 128 + 8 * c + 4] = v4f{acc[4], acc[5], acc[6], acc[7]};
    if (c == 0) {
      lds[L_M + sw] = m;
      lds[L_L + sw] = l;
    }
  }
  wg_barrier();
  {
    const int base = WS_OP + (h * 8 + (lane >> 3)) * OP_BYTES;
    const int oo = base + sw * 128 + (lane & 7) * 16, mo = base + 1024;
    v4u og = __builtin_amdgcn_raw_buffer_load_b128(rs_ws, oo, 0, AUX_SC1);
    v4u ml = __builtin_amdgcn_raw_buffer_load_b128(rs_ws, mo, 0, AUX_SC1);
    unsigned spins = 0;
    for (;;) {
      const bool bad = ((og[1] ^ tagA) | (og[3] ^ tagB) | (ml[1] ^ tagA) | (ml[3] ^ tagB)) != 0u;
      const u64 miss = __ballot(bad);
      if (miss == 0ull || ++spins > SPIN_CAP) break;
      asm volatile("" ::: "memory");
      og = __builtin_amdgcn_raw_buffer_load_b128(rs_ws, oo, 0, AUX_SC1);
      ml = __builtin_amdgcn_raw_buffer_load_b128(rs_ws, mo, 0, AUX_SC1);
    }
    const float mi = __uint_as_float(ml[0]), li = __uint_as_float(ml[2]);
    const float M = x16_max(fmaxf(mi, dpp<XOR8>(mi)));
    const float e = __builtin_amdgcn_exp2f(mi - M);
    float L = li * e, o0 = __uint_as_float(og[0]) * e, o1 = __uint_as_float(og[2]) * e;
    L = x16_sum(L + dpp<XOR8>(L));
    o0 = x16_sum(o0 + dpp<XOR8>(o0));
    o1 = x16_sum(o1 + dpp<XOR8>(o1));
    const float inv = 1.f / L;
    const unsigned opk = pack_bf16(o0 * inv, o1 * inv);
    float y0 = 0.f, y1 = 0.f;
#pragma unroll
    for (int p = 0; p < 8; ++p) {
      y0 = dot2(wp[p], __builtin_amdgcn_readlane(opk, p), y0);
      y1 = dot2(wp[p], __builtin_amdgcn_readlane(opk, 32 + p), y1);
    }
    lds[L_YP + sw * 128 + lane] = y0;
    lds[L_YP + sw * 128 + 64 + lane] = y1;
  }
  wg_barrier();
  if (!is_fin || sw != 0) return;

  {
    float a0, a1;
    gather8<1024>(rs_ws, WS_Y + f * (NH * 256) + lane16, tagA, tagB, a0, a1);
    a0 = xrow_sum(a0);
    a1 = xrow_sum(a1);
    if (lane < 16) __hip_atomic_store((GAS unsigned*)out + fb * (DM / 2) + cg * 16 + lane, pack_bf16(a0, a1), RLX_AGENT);
    if (f == 0 && lane == 0)
      __hip_atomic_store((GAS u64*)ws, ((u64)__float_as_uint(lds[L_TAG + 2]) << 32) | tagA, RLX_AGENT);
  }
}

extern "C" void kernel_launch(void* const* d_in, const int* in_sizes, int n_in, void* d_out, int out_size, void* d_ws,
                              size_t ws_size, hipStream_t stream) {
  (void)in_sizes; (void)n_in; (void)out_size; (void)ws_size;
  fused_decode_kernel<<<NWG, NTHR, 0, stream>>>((const u16*)d_in[0], (const u16*)d_in[1], (const u16*)d_in[2],
                                                (const u16*)d_in[3], (const u16*)d_in[4], (const u16*)d_in[6],
                                                (const u16*)d_in[7], (const u16*)d_in[8], (u16*)d_out,
                                                (unsigned char*)d_ws);
}
```
